```python
import torch

def _softmax(x, axis=-1):
    return torch.softmax(x, dim=axis)
def _gelu(x):
    return torch.nn.functional.gelu(x, approximate="tanh")
def _silu(x):
    return torch.nn.functional.silu(x)
def _sigmoid(x):
    return torch.sigmoid(x)
def _layernorm(x, axis=-1, eps=1e-5):
    m = x.mean(dim=axis, keepdim=True)
    v = x.var(dim=axis, keepdim=True, unbiased=False)
    return (x - m) / torch.sqrt(v + eps)
def _scatter_add(tgt, idx, src, axis=0):
    return tgt.clone().index_add_(axis, idx, src)
def _segment_sum(src, seg, n):
    out = torch.zeros((n,) + src.shape[1:], dtype=src.dtype)
    return out.index_add_(0, seg, src)
def _pad(x, pw):
    flat = tuple(v for pair in reversed(pw) for v in pair)
    return torch.nn.functional.pad(x, flat)

kernel_name = 'recurrent_scan__sliding_block_cached__mixer_channel_mlp__decode'

def setup_inputs():

    _w = (lambda a: (lambda t: t.to(torch.bfloat16) if t.is_floating_point() else t)(torch.as_tensor(a)))
    def _A_setup():
        B, Sq, D = 1, 1, 512
        Hq, Hkv, Sk, Dh = 32, 32, 32768, 128
        assert Hq % Hkv == 0, (Hq, Hkv)
        return {
            "x": _w((lambda *s: torch.randn(*s, dtype=torch.bfloat16) * 0.1)(B, Sq, D)),
            "k_cache": _w((lambda *s: torch.randn(*s, dtype=torch.bfloat16) * 0.8)(B, Hkv, Sk, Dh)),
            "v_cache": _w((lambda *s: torch.randn(*s, dtype=torch.bfloat16) * 3.2)(B, Hkv, Sk, Dh)),
            "wq": _w((lambda *s: torch.randn(*s, dtype=torch.bfloat16) * 0.8)(D, Hq * Dh) * (D ** -0.5)),
            "wo": _w((lambda *s: torch.randn(*s, dtype=torch.bfloat16) * 3.2)(Hq * Dh, D) * ((Hq * Dh) ** -0.5)),
        }
    _A_in = _A_setup()

    def _B_setup():
        B, S, D, F = 1, 1, 512, 1376
        return {
            "x": _w((lambda *s: torch.randn(*s, dtype=torch.bfloat16) * 0.1)(B, S, D)),
            "w1": _w((lambda *s: torch.randn(*s, dtype=torch.bfloat16) * 3.2)(D, F) * (D ** -0.5)),
            "b1": _w((lambda *s: torch.randn(*s, dtype=torch.bfloat16) * 3.2)(F)),
            "w2": _w((lambda *s: torch.randn(*s, dtype=torch.bfloat16) * 3.2)(F, D) * (F ** -0.5)),
            "b2": _w((lambda *s: torch.randn(*s, dtype=torch.bfloat16) * 3.2)(D)),
        }
    _B_in = _B_setup()

    return {
        "A_x": _A_in["x"],
        "A_k_cache": _A_in["k_cache"],
        "A_v_cache": _A_in["v_cache"],
        "A_wq": _A_in["wq"],
        "A_wo": _A_in["wo"],
        "B_x": _B_in["x"],
        "B_w1": _B_in["w1"],
        "B_b1": _B_in["b1"],
        "B_w2": _B_in["w2"],
        "B_b2": _B_in["b2"],
    }

def reference(A_x, A_k_cache, A_v_cache, A_wq, A_wo, B_x, B_w1, B_b1, B_w2, B_b2):
    def _A_ref(x, k_cache, v_cache, wq, wo):
        B, Sq, D = x.shape
        Hq, Dh = 32, 128
        Hkv, Sk = k_cache.shape[1], k_cache.shape[2]
        W = min(4096, max(256, Sk // 4))
        rep = Hq // Hkv
        q = (lambda x, *d: x.permute(*d))((x @ wq).reshape(B, Sq, Hq, Dh), 0, 2, 1, 3)
        k = (lambda x, n, axis: torch.repeat_interleave(x, n, dim=axis))(k_cache[:, :, Sk - W:, :], rep, axis=1)
        v = (lambda x, n, axis: torch.repeat_interleave(x, n, dim=axis))(v_cache[:, :, Sk - W:, :], rep, axis=1)
        scores = torch.einsum("bhqd,bhkd->bhqk", q, k) * (Dh ** -0.5)
        p = _softmax(scores, axis=-1)
        o = torch.einsum("bhqk,bhkd->bhqd", p, v)
        return (lambda x, *d: x.permute(*d))(o, 0, 2, 1, 3).reshape(B, Sq, Hq * Dh) @ wo

    def _B_ref(x, w1, b1, w2, b2):
        y = _layernorm(x, axis=-1)
        h = _gelu(torch.einsum("bsd,df->bsf", y, w1) + b1)
        return torch.einsum("bsf,fd->bsd", h, w2) + b2

    x = A_x
    xn = _layernorm(x, axis=-1)
    s = _A_ref(xn, A_k_cache, A_v_cache, A_wq, A_wo)
    g = _silu(_B_ref(xn, B_w1, B_b1, B_w2, B_b2))
    return x + s * g
```

```cpp
#include <hip/hip_runtime.h>
#include <hip/hip_bf16.h>
#include <stdint.h>

typedef unsigned short u16;
__device__ __forceinline__ float bf2f(u16 b) { return __uint_as_float(((unsigned)b) << 16); }
__device__ __forceinline__ float rbf(float f) { return __bfloat162float(__float2bfloat16(f)); }

constexpr int D = 512, HQ = 32, DH = 128, SK = 32768, W = 4096, F = 1376;

__device__ float block_sum(float v, float* red) {
  for (int o = 32; o > 0; o >>= 1) v += __shfl_xor(v, o);
  int w = threadIdx.x >> 6, nw = blockDim.x >> 6;
  __syncthreads();
  if ((threadIdx.x & 63) == 0) red[w] = v;
  __syncthreads();
  float s = 0; for (int i = 0; i < nw; ++i) s += red[i];
  return s;
}

__device__ void ln512(const u16* x, float* xn, float* y, float* red) {
  int t = threadIdx.x;
  float v = bf2f(x[t]);
  float m = block_sum(v, red) * (1.f / D);
  float d = v - m;
  float var = block_sum(d * d, red) * (1.f / D);
  float a = rbf(d / sqrtf(var + 1e-5f));
  xn[t] = a;
  float m2 = block_sum(a, red) * (1.f / D);
  float d2 = a - m2;
  float var2 = block_sum(d2 * d2, red) * (1.f / D);
  y[t] = rbf(d2 / sqrtf(var2 + 1e-5f));
  __syncthreads();
}

__global__ void __launch_bounds__(512) k1(const u16* x, const u16* wq, const u16* w1, const u16* b1, float* q, float* h) {
  __shared__ float xn[D], y[D], red[16];
  ln512(x, xn, y, red);
  int col = blockIdx.x * 512 + threadIdx.x;
  if (col < HQ * DH) {
    float s = 0; for (int k = 0; k < D; ++k) s += xn[k] * bf2f(wq[(size_t)k * (HQ * DH) + col]);
    q[col] = rbf(s);
  } else {
    int f = col - HQ * DH;
    if (f < F) {
      float s = 0; for (int k = 0; k < D; ++k) s += y[k] * bf2f(w1[(size_t)k * F + f]);
      s = rbf(rbf(s) + bf2f(b1[f]));
      float g = 0.5f * s * (1.f + tanhf(0.7978845608028654f * (s + 0.044715f * s * s * s)));
      h[f] = rbf(g);
    }
  }
}

__global__ void __launch_bounds__(512) k2(const u16* kc, const u16* vc, const float* q, float* po, float* pml) {
  __shared__ float p[512], red[16], acc[4][128];
  int hh = blockIdx.x >> 3, s = blockIdx.x & 7, t = threadIdx.x;
  const u16* kp = kc + ((size_t)hh * SK + (SK - W) + s * 512 + t) * DH;
  float sc = 0; for (int d = 0; d < DH; ++d) sc += q[hh * DH + d] * bf2f(kp[d]);
  sc *= 0.08838834764831845f;
  float m = sc; for (int o = 32; o > 0; o >>= 1) m = fmaxf(m, __shfl_xor(m, o));
  if ((t & 63) == 0) red[t >> 6] = m;
  __syncthreads();
  m = red[0]; for (int i = 1; i < 8; ++i) m = fmaxf(m, red[i]);
  float e = __expf(sc - m);
  p[t] = e;
  float l = block_sum(e, red);
  const u16* vp = vc + ((size_t)hh * SK + (SK - W) + s * 512) * DH;
  int d = t & 127, g = t >> 7; float a = 0;
  for (int k = g * 128; k < g * 128 + 128; ++k) a += p[k] * bf2f(vp[(size_t)k * DH + d]);
  acc[g][d] = a;
  __syncthreads();
  if (t < 128) po[blockIdx.x * 128 + t] = acc[0][t] + acc[1][t] + acc[2][t] + acc[3][t];
  if (t == 0) { pml[blockIdx.x * 2] = m; pml[blockIdx.x * 2 + 1] = l; }
}

__global__ void __launch_bounds__(512) k3(const u16* x, const u16* wo, const u16* w2, const u16* b2, const float* po, const float* pml, const float* h, u16* out) {
  __shared__ float o[HQ * DH]; __shared__ float part[8][64], part2[8][64];
  int t = threadIdx.x;
  for (int i = t; i < HQ * DH; i += 512) {
    int hh = i >> 7, d = i & 127; float M = -INFINITY;
    for (int s = 0; s < 8; ++s) M = fmaxf(M, pml[(hh * 8 + s) * 2]);
    float L = 0, a = 0;
    for (int s = 0; s < 8; ++s) { float w = __expf(pml[(hh * 8 + s) * 2] - M); L += w * pml[(hh * 8 + s) * 2 + 1]; a += w * po[(hh * 8 + s) * 128 + d]; }
    o[i] = rbf(a / L);
  }
  __syncthreads();
  int c = blockIdx.x * 64 + (t & 63), g = t >> 6;
  float s = 0; for (int k = g * 512; k < g * 512 + 512; ++k) s += o[k] * bf2f(wo[(size_t)k * D + c]);
  float s2 = 0; for (int k = g * 172; k < g * 172 + 172; ++k) s2 += h[k] * bf2f(w2[(size_t)k * D + c]);
  part[g][t & 63] = s; part2[g][t & 63] = s2;
  __syncthreads();
  if (t < 64) {
    float a = 0, b = 0; for (int i = 0; i < 8; ++i) { a += part[i][t]; b += part2[i][t]; }
    a = rbf(a); b = rbf(rbf(b) + bf2f(b2[c]));
    float gt = rbf(b / (1.f + __expf(-b)));
    float r = bf2f(x[c]) + rbf(a * gt);
    __hip_bfloat16 o16 = __float2bfloat16(r);
    out[c] = *(u16*)&o16;
  }
}

extern "C" void kernel_launch(void* const* d_in, const int* in_sizes, int n_in, void* d_out, int out_size, void* d_ws, size_t ws_size, hipStream_t stream) {
  const u16* x = (const u16*)d_in[0]; const u16* kc = (const u16*)d_in[1]; const u16* vc = (const u16*)d_in[2];
  const u16* wq = (const u16*)d_in[3]; const u16* wo = (const u16*)d_in[4];
  const u16* w1 = (const u16*)d_in[6]; const u16* b1 = (const u16*)d_in[7]; const u16* w2 = (const u16*)d_in[8]; const u16* b2 = (const u16*)d_in[9];
  float* ws = (float*)d_ws; float* q = ws; float* h = ws + 4096; float* po = ws + 8192; float* pml = po + 256 * 128;
  k1<<<11, 512, 0, stream>>>(x, wq, w1, b1, q, h);
  k2<<<256, 512, 0, stream>>>(kc, vc, q, po, pml);
  k3<<<8, 512, 0, stream>>>(x, wo, w2, b2, po, pml, h, (u16*)d_out);
}
```

```cpp
#include <hip/hip_runtime.h>
#include <stdint.h>

typedef unsigned u32;
typedef unsigned long long u64;
typedef unsigned short u16;
typedef u32 v16u __attribute__((ext_vector_type(16)));
typedef u32 v4u __attribute__((ext_vector_type(4)));
typedef u32 v2u __attribute__((ext_vector_type(2)));
typedef float f4 __attribute__((ext_vector_type(4)));
typedef float f2 __attribute__((ext_vector_type(2)));
typedef short s4v __attribute__((ext_vector_type(4)));
typedef __bf16 bf8v __attribute__((ext_vector_type(8)));
typedef __bf16 bf2v __attribute__((ext_vector_type(2)));
typedef __amdgpu_buffer_rsrc_t rsrc_t;
#define LAS __attribute__((address_space(3)))
#define GAS __attribute__((address_space(1)))

constexpr int NS = 8;
constexpr int NTHR = (NS + 1) * 64;
constexpr int NL = 16;
constexpr int NG = NL / 4;
constexpr int WIN = 6;
constexpr int N0 = 6;
constexpr int TAU = 452;
constexpr int LAT = 34;
constexpr int CAP = 40;
constexpr int CPF = 11;

constexpr int AUX_NT = 2, AUX_SC1 = 16;
constexpr u32 OOB = 1u << 30;
constexpr u32 SPIN_CAP = 1u << 17;
constexpr u32 TM = 0x3ffu, EP_STEP = 0x9E3779F8u;
constexpr u32 TG_A = 0x11u, TG_B = 0x123u, TG_C = 0x275u, TG_D = 0x339u, TW_0 = 0x7F4A7C15u, TW_1 = 0x5851F42Du;

constexpr int WS_EPOCH = 0, WS_H1 = 4096, H1_STR = 640, WS_H0 = 262144, WS_END = WS_H0 + 262144;
constexpr int L_Q = 0, L_ML = 256, L_EP = 320, L_QF = 336, L_CNT = 340, L_WF = 344, L_OF = 348, L_SM = 352, L_XS = 512, L_YS = 1536, L_HS = 2560,
              L_OT = 2688, L_HP = 2816, L_XR = 3072, L_OW = 4096, L_WO = L_OW + NS * 512, L_W2 = L_WO + 16384, L_END = L_W2 + 6144;

__device__ __forceinline__ rsrc_t mkrsrc(const void* p, u32 bytes) { return __builtin_amdgcn_make_buffer_rsrc((void*)p, 0, bytes, 0x00020000); }
__device__ __forceinline__ v4u mkdesc(const void* p, u32 bytes) { return v4u{(u32)(u64)p, (u32)((u64)p >> 32) & 0xffffu, bytes, 0x00020000u}; }
template <int AUX> __device__ __forceinline__ v4u ld16(rsrc_t r, u32 v, u32 s) { return __builtin_amdgcn_raw_buffer_load_b128(r, v, s, AUX); }
template <int AUX> __device__ __forceinline__ v2u ld8(rsrc_t r, u32 v, u32 s) { return __builtin_amdgcn_raw_buffer_load_b64(r, v, s, AUX); }
__device__ __forceinline__ void st16(rsrc_t r, u32 v, u32 s, v4u x) { __builtin_amdgcn_raw_buffer_store_b128(x, r, v, s, AUX_SC1); }
__device__ __forceinline__ void dma16(v4u rs, u32 voff, u32 soff, u32 lds_addr) {
  asm volatile("s_mov_b32 m0, %3\n\ts_nop 0\n\tbuffer_load_dwordx4 %0, %1, %2 offen nt lds" :: "v"(voff), "s"(rs), "s"(soff), "s"(lds_addr) : "memory");
}
__device__ __forceinline__ v2u trd(LAS unsigned char* p) { return __builtin_bit_cast(v2u, __builtin_amdgcn_ds_read_tr16_b64_v4i16((LAS s4v*)p)); }
__device__ __forceinline__ f4 mfma(v4u a, v4u b, f4 c) {
  return __builtin_amdgcn_mfma_f32_16x16x32_bf16(__builtin_bit_cast(bf8v, a), __builtin_bit_cast(bf8v, b), c, 0, 0, 0);
}
__device__ __forceinline__ float u2f(u32 x) { return __builtin_bit_cast(float, x); }
__device__ __forceinline__ u32 f2u(float x) { return __builtin_bit_cast(u32, x); }
__device__ __forceinline__ u32 pk(float lo, float hi) { bf2v r = {(__bf16)lo, (__bf16)hi}; return __builtin_bit_cast(u32, r); }
__device__ __forceinline__ float rbf(float x) { return (float)(__bf16)x; }
__device__ __forceinline__ float lo16(u32 x) { return u2f(x << 16); }
__device__ __forceinline__ float hi16(u32 x) { return u2f(x & 0xffff0000u); }
__device__ __forceinline__ u32 enc(float v, u32 tag) { return (f2u(v) & ~TM) | (tag & TM); }
__device__ __forceinline__ float dec(u32 w) { return u2f(w & ~TM); }
__device__ __forceinline__ u32 perm(u32 p, u32 sel) { return __builtin_amdgcn_perm(p, p, sel); }
template <int CTRL> __device__ __forceinline__ u32 dppu(u32 x) { return (u32)__builtin_amdgcn_mov_dpp((int)x, CTRL, 0xf, 0xf, true); }
template <int CTRL> __device__ __forceinline__ float dpp(float x) { return u2f(dppu<CTRL>(f2u(x))); }
constexpr int QX1 = 0xB1, QX2 = 0x4E, HMIR = 0x141, MIR = 0x140, ROR4 = 0x124, ROR8 = 0x128, BCAST = 0x150;
__device__ __forceinline__ float half_sum(float x) { auto t = __builtin_amdgcn_permlane32_swap(f2u(x), f2u(x), false, false); return u2f(t[0]) + u2f(t[1]); }
__device__ __forceinline__ float half_max(float x) { auto t = __builtin_amdgcn_permlane32_swap(f2u(x), f2u(x), false, false); return fmaxf(u2f(t[0]), u2f(t[1])); }
__device__ __forceinline__ float rows_sum(float x) {
  auto t = __builtin_amdgcn_permlane16_swap(f2u(x), f2u(x), false, false);
  return half_sum(u2f(t[0]) + u2f(t[1]));
}
__device__ __forceinline__ float rows_max(float x) {
  auto t = __builtin_amdgcn_permlane16_swap(f2u(x), f2u(x), false, false);
  return half_max(fmaxf(u2f(t[0]), u2f(t[1])));
}
__device__ __forceinline__ float wave_sum(float x) {
  x += dpp<QX1>(x); x += dpp<QX2>(x); x += dpp<HMIR>(x); x += dpp<MIR>(x);
  return rows_sum(x);
}
__device__ __forceinline__ float oct_sum(float x) { x += dpp<QX1>(x); x += dpp<QX2>(x); return x + dpp<HMIR>(x); }
__device__ __forceinline__ float oct_max(float x) { x = fmaxf(x, dpp<QX1>(x)); x = fmaxf(x, dpp<QX2>(x)); return fmaxf(x, dpp<HMIR>(x)); }
template <int N> __device__ __forceinline__ void wait_vm() { asm volatile("s_waitcnt vmcnt(%0)" :: "n"(N) : "memory"); }
__device__ __forceinline__ u32 now32() { u64 t; asm volatile("s_memrealtime %0\n\ts_waitcnt lgkmcnt(0)" : "=s"(t) :: "memory"); return (u32)t; }
__device__ __forceinline__ void pace(u32& t, int step) {
  asm volatile("s_addk_i32 %0, %1\n"
               ".Lpace%=:\n\ts_sleep 1\n\ts_memrealtime vcc\n\ts_waitcnt lgkmcnt(0)\n\ts_sub_i32 vcc_lo, vcc_lo, %0\n\ts_cmp_lt_i32 vcc_lo, 0\n\ts_cbranch_scc1 .Lpace%=\n\t"
               "s_addk_i32 vcc_lo, %2\n\ts_max_i32 vcc_lo, vcc_lo, 0\n\ts_add_i32 %0, %0, vcc_lo"
               : "+s"(t) : "i"(step), "i"(-CAP) : "memory", "scc", "vcc");
}
constexpr int tstep(int n) { return ((n * TAU) >> 4) - (((n - 1) * TAU) >> 4); }

#define SB() __builtin_amdgcn_sched_barrier(0)
#define CBAR() asm volatile("" ::: "memory")
#define OPAQUE(x) asm volatile("" : "+v"(x))
#define WG_BARRIER() do { SB(); asm volatile("s_waitcnt lgkmcnt(0)\n\ts_barrier" ::: "memory"); SB(); } while (0)
#define LDS_SET(off) do { CBAR(); *(volatile LAS u32*)(lds + (off)) = 1u; CBAR(); SB(); } while (0)
#define LDS_ARRIVE(off) do { CBAR(); if (lane == 0) __hip_atomic_fetch_add((LAS u32*)(lds + (off)), 1u, __ATOMIC_RELAXED, __HIP_MEMORY_SCOPE_WORKGROUP); CBAR(); SB(); } while (0)
__device__ __forceinline__ void lds_wait(u32 addr, int val) {
  u32 v, n = SPIN_CAP << 6;
  asm volatile(".Lw%=:\n\tds_read_b32 %0, %2\n\ts_sub_u32 %1, %1, 1\n\ts_cbranch_scc1 .Lx%=\n\ts_waitcnt lgkmcnt(0)\n\t"
               "v_cmp_gt_u32 vcc, %3, %0\n\ts_cbranch_vccnz .Lw%=\n.Lx%=:\n\ts_waitcnt lgkmcnt(0)"
               : "=&v"(v), "+s"(n) : "v"(addr), "i"(val) : "memory", "scc", "vcc");
}
#define LDS_WAIT(off, val) do { SB(); lds_wait((u32)(u64)lds + (off), val); SB(); } while (0)
#define ANY(x) (__builtin_amdgcn_ballot_w64((x) != 0) != 0)

__global__ void __launch_bounds__(NTHR, 3)
fused_decode(const u16* x_, const u16* wq_, const u16* w1_, unsigned char* ws_, const u16* kc_, const u16* vc_, const u16* wo_, const u16* w2_,
             const u16* b1_, const u16* b2_, u16* out_) {
  __shared__ __attribute__((aligned(16))) unsigned char lds_raw[L_END];
  LAS unsigned char* lds = (LAS unsigned char*)lds_raw;
  v16u ka; v4u kb; u64 kc2;
  asm volatile("s_load_dwordx16 %0, %3, 0x0\n\ts_load_dwordx4 %1, %3, 0x40\n\ts_load_dwordx2 %2, %3, 0x50"
               : "=&s"(ka), "=&s"(kb), "=&s"(kc2) : "s"(__builtin_amdgcn_kernarg_segment_ptr()));
#define RAW(v, i) (((u64)v[(i) + 1] << 32) | v[i])
#define GP(T, r) ((T)(GAS void*)(r))
  const int b = blockIdx.x, lane = threadIdx.x & 63, wave = __builtin_amdgcn_readfirstlane(threadIdx.x >> 6);
  const int xg = b & 7, j = b >> 3, h = xg * 4 + (j >> 3), s = j & 7;
  const int g = lane >> 4, c = lane & 15;
  const int cb = s & 3, rh = s >> 2;
  constexpr float C2 = 0.08838834764831845f * 1.4426950408889634f;

  if (wave == 0) {
    __builtin_amdgcn_s_setprio(3);
    u64 r0, r1, r2, r3, r4, r5, r6, r7, r8;
    asm volatile("s_waitcnt lgkmcnt(0)\n\ts_mov_b64 %0, %9\n\ts_mov_b64 %1, %10\n\ts_mov_b64 %2, %11\n\ts_mov_b64 %3, %12\n\ts_mov_b64 %4, %13\n\t"
                 "s_mov_b64 %5, %14\n\ts_mov_b64 %6, %15\n\ts_mov_b64 %7, %16\n\ts_mov_b64 %8, %17"
                 : "=&s"(r0), "=&s"(r1), "=&s"(r2), "=&s"(r3), "=&s"(r4), "=&s"(r5), "=&s"(r6), "=&s"(r7), "=&s"(r8)
                 : "s"(RAW(ka, 0)), "s"(RAW(ka, 2)), "s"(RAW(ka, 4)), "s"(RAW(ka, 6)), "s"(RAW(ka, 12)), "s"(RAW(ka, 14)),
                   "s"(RAW(kb, 0)), "s"(RAW(kb, 2)), "s"(kc2), "s"(ka), "s"(kb));
    const u16 *x = GP(const u16*, r0), *wq = GP(const u16*, r1), *w1 = GP(const u16*, r2), *wo = GP(const u16*, r4), *w2 = GP(const u16*, r5),
              *b1 = GP(const u16*, r6), *b2 = GP(const u16*, r7);
    unsigned char* ws = GP(unsigned char*, r3);
    u16* out = GP(u16*, r8);
    const rsrc_t rws = mkrsrc(ws, WS_END);
    SB();
    const int nh = h < 12 ? 6 : 5, c0 = 5 * h + (h < 12 ? h : 12);
    const int cbc = (cb + 1) & 3, Lc = 8 * xg + 2 * (j >> 3) + rh, col = 128 * cbc + 2 * Lc;
    const u32 slot0 = WS_H0 + (h * 8 + s) * 1024, slot1 = WS_H1 + (h * 8 + s) * H1_STR;
    const v4u xv = *(const v4u*)(x + lane * 8);
    v4u WQ[16]; v2u W1[16];
    {
      const rsrc_t rq = mkrsrc(wq + (size_t)s * 64 * 4096 + h * 128, 64 * 8192);
      const rsrc_t r1_ = mkrsrc(w1 + (size_t)s * 64 * 1376 + c0 * 8, 64 * 2752);
      const u32 vq = g * 16 * 8192 + c * 16, v1 = c < 2 * nh ? g * 16 * 2752 + c * 8 : OOB;
#pragma unroll
      for (int i = 0; i < 16; ++i) WQ[i] = ld16<AUX_NT>(rq, vq, i * 8192);
      SB();
#pragma unroll
      for (int i = 0; i < 16; ++i) W1[i] = ld8<AUX_NT>(r1_, v1, i * 2752);
      SB();
    }
    if (lane < 5) *(volatile LAS u32*)(lds + L_QF + lane * 4) = 0u;
    WG_BARRIER();
    const u32 epoch = __builtin_amdgcn_raw_buffer_load_b32(rws, WS_EPOCH, 0, AUX_SC1);
    const u32 b1v = __builtin_amdgcn_raw_buffer_load_b16(mkrsrc(b1 + c0 * 8, nh * 16), (4 * c + g) * 2, 0, AUX_NT);
    const u32 b2v = __builtin_amdgcn_raw_buffer_load_b16(mkrsrc(b2 + col, 4), (lane & 1) * 2, 0, AUX_NT);
    SB();
    st16(rws, lane * 16, slot0, v4u{0u, 1u, 2u, 3u});
    st16(rws, lane < 40 ? lane * 16 : OOB, slot1, v4u{0u, 1u, 2u, 3u});
    SB();
    {
      float v[8];
#pragma unroll
      for (int k = 0; k < 4; ++k) { v[2 * k] = lo16(xv[k]); v[2 * k + 1] = hi16(xv[k]); }
      *(LAS v4u*)(lds + L_XR + lane * 16) = xv;
      const float piv = u2f(__builtin_amdgcn_readfirstlane(f2u(v[0])));
#pragma unroll
      for (int k = 0; k < 8; ++k) v[k] -= piv;
#pragma unroll
      for (int pass = 0; pass < 2; ++pass) {
        float a = 0.f, q2 = 0.f;
#pragma unroll
        for (int k = 0; k < 8; ++k) { a += v[k]; q2 = fmaf(v[k], v[k], q2); }
        const float mean = wave_sum(a) * (1.f / 512.f), var = fmaxf(fmaf(-mean, mean, wave_sum(q2) * (1.f / 512.f)), 0.f);
        const float rs = __builtin_amdgcn_rsqf(var + 1e-5f);
        v4u o;
#pragma unroll
        for (int k = 0; k < 4; ++k) { o[k] = pk((v[2 * k] - mean) * rs, (v[2 * k + 1] - mean) * rs); v[2 * k] = lo16(o[k]); v[2 * k + 1] = hi16(o[k]); }
        *(LAS v4u*)(lds + (pass ? L_YS : L_XS) + lane * 16) = o;
      }
    }
    SB();
    v4u own;
    {
      const v4u xa = *(LAS v4u*)(lds + L_XS + (64 * s + 16 * g) * 2), xb = *(LAS v4u*)(lds + L_XS + (64 * s + 16 * g) * 2 + 16);
      const v4u ya = *(LAS v4u*)(lds + L_YS + (64 * s + 16 * g) * 2), yb = *(LAS v4u*)(lds + L_YS + (64 * s + 16 * g) * 2 + 16);
      u32 sel[2][4];
#pragma unroll
      for (int p = 0; p < 2; ++p)
#pragma unroll
        for (int q = 0; q < 4; ++q)
          sel[p][q] = ((c & 2) == 0 && (c >> 2) == q) ? ((c & 1) ? (0x01000c0cu + p * 0x02020000u) : (0x0c0c0100u + p * 0x0202u)) : 0x0c0c0c0cu;
      f4 aq = {0.f, 0.f, 0.f, 0.f}, ah = {0.f, 0.f, 0.f, 0.f};
#pragma unroll
      for (int i = 0; i < 16; ++i) {
        const u32 P = i < 8 ? xa[i >> 1] : xb[(i >> 1) - 4];
        aq = mfma(v4u{perm(P, sel[i & 1][0]), perm(P, sel[i & 1][1]), perm(P, sel[i & 1][2]), perm(P, sel[i & 1][3])}, WQ[i], aq);
      }
      const u32 slo = (c & 4) ? 0x01000c0cu : 0x0c0c0100u, shi = (c & 4) ? 0x03020c0cu : 0x0c0c0302u;
      const bool on0 = (c & 11) == 0, on1 = (c & 11) == 8;
      const u32 s0 = on0 ? slo : 0x0c0c0c0cu, s1 = on1 ? slo : 0x0c0c0c0cu, s2 = on0 ? shi : 0x0c0c0c0cu, s3 = on1 ? shi : 0x0c0c0c0cu;
#pragma unroll
      for (int i = 0; i < 8; ++i) {
        const u32 P = i < 4 ? ya[i] : yb[i - 4];
        ah = mfma(v4u{perm(P, s0), perm(P, s1), perm(P, s2), perm(P, s3)}, v4u{W1[2 * i][0], W1[2 * i][1], W1[2 * i + 1][0], W1[2 * i + 1][1]}, ah);
      }
      own = v4u{enc(aq[0], epoch + TG_A), enc(aq[1], epoch + TG_A), enc(ah[0], epoch + TG_A), epoch + TW_0};
      st16(rws, lane * 16, slot0, own);
    }
    SB();
    __builtin_amdgcn_s_sleep(60);
    float hpre;
    {
      v4u a[8];
      for (u32 spin = 0;; ++spin) {
        u32 bad = 0;
#pragma unroll
        for (int k = 0; k < 8; ++k) a[k] = ld16<AUX_SC1>(rws, k == s ? OOB : lane * 16, WS_H0 + (h * 8 + k) * 1024);
#pragma unroll
        for (int k = 0; k < 8; ++k) {
          a[k] = k == s ? own : a[k];
          bad |= (((a[k][0] ^ own[0]) | (a[k][1] ^ own[0]) | (a[k][2] ^ own[0])) & TM) | (a[k][3] ^ own[3]);
          OPAQUE(bad);
        }
        CBAR();
        if (!ANY(bad) || spin > SPIN_CAP) break;
        __builtin_amdgcn_s_sleep(16);
      }
      float q0 = 0.f, q1 = 0.f; hpre = 0.f;
#pragma unroll
      for (int k = 0; k < 8; ++k) { q0 += dec(a[k][0]); q1 += dec(a[k][1]); hpre += dec(a[k][2]); }
      *(LAS u32*)(lds + L_Q + 16 * c + 4 * g) = pk(q0, q1);
      if (lane == 0) *(LAS u32*)(lds + L_EP) = epoch;
      LDS_SET(L_QF);
    }
    {
      const float u = rbf(rbf(hpre) + lo16(b1v));
      const float z = 1.5957691216057308f * (u + 0.044715f * u * u * u);
      const float ge = u * __builtin_amdgcn_rcpf(1.f + __expf(-z));
      *(LAS u16*)(lds + L_HS + (4 * c + g) * 2) = (u16)pk(ge, ge);
      const u16 t = *(LAS u16*)(lds + L_HS + (rh * 4 * nh + (lane & 31)) * 2);
      *(LAS u16*)(lds + L_HP + (lane & 31) * 2) = (lane & 31) < 4 * nh ? t : (u16)0;
    }
    const float xo = lo16(*(LAS u16*)(lds + L_XR + (col + (lane & 1)) * 2));
    const v4u rw2 = mkdesc(w2 + (size_t)(8 * c0 + rh * 4 * nh) * 512 + cb * 128, 24 * 1024);
    const v4u rwo = mkdesc(wo + (size_t)(128 * h + 64 * rh) * 512 + cb * 128, 64 * 1024);
    const v4u rwd = mkdesc(ws, WS_END);
    const u32 vw0 = g * 1024 + (c ^ (2 * g)) * 16, vw1 = vw0 ^ 128;
    const int hf = lane >> 5, msp = 2 * (lane & 3) + hf;
    const u32 mloff = msp == s ? OOB : msp * H1_STR;
    u32 o1v[4];
#pragma unroll
    for (int k = 0; k < 4; ++k) { o1v[k] = (2 * k + hf == s) ? OOB : hf * H1_STR + 128 + rh * 256 + (lane & 31) * 8; OPAQUE(o1v[k]); }
    const u32 tgo = (epoch + TG_B) & TM;
    u32 yoff = ((lane >> 1) * 8 + (lane & 1) * 4 + cbc) * 1024 + Lc * 16;
    OPAQUE(yoff);
    const u32 tc = epoch + TG_C, td = epoch + TG_D;
    const u32 ldsb = (u32)(u64)lds;
    LDS_WAIT(L_SM, NS);
    v4u mlo;
    {
      const f2 mlw = *(LAS f2*)(lds + L_ML + (lane & 7) * 8);
      const float M = oct_max(mlw[0]), L = oct_sum(mlw[1] * __builtin_amdgcn_exp2f((mlw[0] - M) * C2));
      mlo = v4u{f2u(M), epoch + TW_0, f2u(L), epoch + TW_1};
      st16(rws, lane == 0 ? 0u : OOB, slot1, mlo);
    }
    WG_BARRIER();
    __builtin_amdgcn_s_sleep(4);
#define WOPACE(i) do { if ((i) & 1) __builtin_amdgcn_s_sleep(2); else __builtin_amdgcn_s_sleep(1); } while (0)
#pragma unroll
    for (int i = 0; i < 6; ++i) { if (i < 5 || nh == 6) dma16(rw2, (i & 2) ? vw1 : vw0, i * 4096, ldsb + L_W2 + i * 1024); WOPACE(i); }
    v4u ml;
#pragma unroll
    for (int i = 0; i < 16; ++i) {
      if (i == 2) asm volatile("buffer_load_dwordx4 %0, %1, %2, %3 offen sc1" : "=v"(ml) : "v"(mloff), "s"(rwd), "s"(WS_H1 + h * 8 * H1_STR) : "memory");
      dma16(rwo, (i & 2) ? vw1 : vw0, i * 4096, ldsb + L_WO + i * 1024);
      if (i < 15) WOPACE(i);
    }
    asm volatile("s_waitcnt vmcnt(14)" : "+v"(ml) :: "memory");
    SB();
    if (msp == s) ml = mlo;
    if (__builtin_expect(ANY((ml[1] ^ mlo[1]) | (ml[3] ^ mlo[3])), 0)) {
      for (u32 spin = 0;; ++spin) {
        ml = ld16<AUX_SC1>(rws, mloff, WS_H1 + h * 8 * H1_STR);
        if (msp == s) ml = mlo;
        CBAR();
        if (!ANY((ml[1] ^ mlo[1]) | (ml[3] ^ mlo[3])) || spin > SPIN_CAP) break;
      }
    }
    LDS_WAIT(L_CNT, NS);
    v2u a[4];
#pragma unroll
    for (int k = 0; k < 4; ++k) a[k] = ld8<AUX_SC1>(rws, o1v[k], WS_H1 + (h * 8 + 2 * k) * H1_STR);
    SB();
    float f0, f1, f2_, f3;
    {
      float MH = u2f(ml[0]);
      MH = fmaxf(MH, dpp<QX1>(MH)); MH = half_max(fmaxf(MH, dpp<QX2>(MH)));
      const float e = __builtin_amdgcn_exp2f((u2f(ml[0]) - MH) * C2);
      float LH = u2f(ml[2]) * e;
      LH += dpp<QX1>(LH); LH = half_sum(LH + dpp<QX2>(LH));
      const float f = e * __builtin_amdgcn_rcpf(LH);
      f0 = dpp<0x00>(f); f1 = dpp<0x55>(f); f2_ = dpp<0xAA>(f); f3 = dpp<0xFF>(f);
      asm volatile("" : "+v"(f0), "+v"(f1), "+v"(f2_), "+v"(f3));
    }
    float p0, p1;
    u32 tmk[4], tgv = tgo, one = 1u, otoff = L_OT + (8 * (lane & 7) + 2 * ((lane >> 3) & 3) + hf) * 2;
    {
      float o0 = 0.f, o1 = 0.f;
      const int src = 16 * ((lane >> 3) & 3) + 8 * rh + (lane & 7);
#pragma unroll
      for (int k = 0; k < NS; ++k) { const f2 t = *(LAS f2*)(lds + L_OW + k * 512 + src * 8); o0 += t[0]; o1 += t[1]; }
      const float fo = hf != (s & 1) ? 0.f : (s >> 1) == 0 ? f0 : (s >> 1) == 1 ? f1 : (s >> 1) == 2 ? f2_ : f3;
      p0 = hf != (s & 1) ? 0.f : fo * o0; p1 = hf != (s & 1) ? 0.f : fo * o1;
#pragma unroll
      for (int k = 0; k < 4; ++k) { tmk[k] = o1v[k] == OOB ? 0u : TM; OPAQUE(tmk[k]); }
      asm volatile("" : "+v"(p0), "+v"(p1), "+v"(tgv), "+v"(one), "+v"(otoff));
    }
    OPAQUE(a[0]);
    CBAR(); *(volatile LAS u32*)(lds + L_WF) = one; CBAR();
    SB();
#define O_BAD() ((((a[0][0] ^ tgv) | (a[0][1] ^ tgv)) & tmk[0]) | (((a[1][0] ^ tgv) | (a[1][1] ^ tgv)) & tmk[1]) | \
                 (((a[2][0] ^ tgv) | (a[2][1] ^ tgv)) & tmk[2]) | (((a[3][0] ^ tgv) | (a[3][1] ^ tgv)) & tmk[3]))
    if (__builtin_expect(ANY(O_BAD()), 0)) {
      for (u32 spin = 0;; ++spin) {
#pragma unroll
        for (int k = 0; k < 4; ++k) a[k] = ld8<AUX_SC1>(rws, o1v[k], WS_H1 + (h * 8 + 2 * k) * H1_STR);
        CBAR();
        if (!ANY(O_BAD()) || spin > SPIN_CAP) break;
      }
    }
    {
      const float o0 = fmaf(f0, dec(a[0][0]), fmaf(f1, dec(a[1][0]), p0)) + fmaf(f2_, dec(a[2][0]), f3 * dec(a[3][0]));
      const float o1 = fmaf(f0, dec(a[0][1]), fmaf(f1, dec(a[1][1]), p1)) + fmaf(f2_, dec(a[2][1]), f3 * dec(a[3][1]));
      auto r = __builtin_amdgcn_permlane32_swap(f2u(o0), f2u(o1), false, false);
      const float os = u2f(r[0]) + u2f(r[1]);
      *(LAS u16*)(lds + otoff) = (u16)pk(os, os);
      CBAR(); *(volatile LAS u32*)(lds + L_OF) = one; CBAR(); SB();
    }
    {
      u32 yo = yoff, tcv = tc, tdv = td, oo = lane == 0 ? 0u : OOB;
      float b2f = lo16(b2v), xr = xo;
      asm volatile("" : "+v"(yo), "+v"(tcv), "+v"(tdv), "+v"(oo), "+v"(b2f), "+v"(xr));
      const rsrc_t rout = mkrsrc(out + col, 4);
      SB();
      __builtin_amdgcn_s_sleep(22);
      v4u y = ld16<AUX_SC1>(rws, yo, WS_H0);
#define Y_BAD() (((y[0] ^ tcv) | (y[1] ^ tcv) | (y[2] ^ tcv) | (y[3] ^ tdv)) & TM)
      if (ANY(Y_BAD())) {
        for (u32 spin = 0;; ++spin) {
          CBAR();
          y = ld16<AUX_SC1>(rws, yo, WS_H0);
          if (!ANY(Y_BAD()) || spin > SPIN_CAP) break;
        }
      }
      SB();
      const bool b0 = lane & 1, b1_ = lane & 2;
      const float ya = dec(b0 ? y[1] : y[0]) + dpp<QX1>(dec(b0 ? y[0] : y[1])), za = dec(b0 ? y[3] : y[2]) + dpp<QX1>(dec(b0 ? y[2] : y[3]));
      float t = (b1_ ? za : ya) + dpp<QX2>(b1_ ? ya : za);
      t += dpp<ROR4>(t); t += dpp<ROR8>(t);
      t = rbf(rows_sum(t));
      const float zz = rbf(t + b2f);
      const float gt = rbf(zz * __builtin_amdgcn_rcpf(1.f + __expf(-zz)));
      const float r = xr + rbf(t * dpp<QX2>(gt));
      __builtin_amdgcn_raw_buffer_store_b32(pk(r, dpp<QX1>(r)), rout, oo, 0, 0);
      if (b == 0) __builtin_amdgcn_raw_buffer_store_b32(epoch + EP_STEP, rws, lane == 0 ? WS_EPOCH : OOB, 0, 0);
    }
    return;
  }

  const int w = wave - 1;
  u32 cpf = 0;
  if (w == NS - 1) {
    const u64 pc = (u64)__builtin_amdgcn_s_getpc() & ~(u64)255;
    cpf = __builtin_amdgcn_raw_buffer_load_b32(mkrsrc((const void*)pc, CPF * 1024), (j % CPF) * 1024 + lane * 16, 0, 0);
  }
  SB();
  u64 r0, r1, r2;
  asm volatile("s_waitcnt lgkmcnt(0)\n\ts_mov_b64 %0, %3\n\ts_mov_b64 %1, %4\n\ts_mov_b64 %2, %5" : "=&s"(r0), "=&s"(r1), "=&s"(r2)
               : "s"(RAW(ka, 6)), "s"(RAW(ka, 8)), "s"(RAW(ka, 10)), "s"(ka), "s"(kb), "s"(kc2));
  unsigned char* ws = GP(unsigned char*, r0);
  const rsrc_t rws = mkrsrc(ws, WS_END);
  const size_t base = ((size_t)h * 32768 + 28672) * 128;
  const rsrc_t rk = mkrsrc(GP(const u16*, r1) + base, 1048576), rv = mkrsrc(GP(const u16*, r2) + base, 1048576);
  const u32 wgo = s * 8192 + w * 1024;
  const u32 koff = wgo + ((lane >> 2) & 3) * 256 + g * 64 + (lane & 3) * 16, voff = wgo + lane * 16;
  u32 sb[NG];
#pragma unroll
  for (int gi = 0; gi < NG; ++gi) sb[gi] = ((gi + h) & 3) * 262144;
#define LDKV(r, vo, n) ld16<AUX_NT>(r, vo, sb[(n) >> 2] + ((n) & 3) * 65536)
  v4u K[NL], V[NL];
  u32 tb = 0;
#define ISSUE_V(n) do { if ((n) < NL) { wait_vm<WIN - 1>(); pace(tb, tstep(NL + (n))); \
    V[n] = LDKV(rv, voff, n); SB(); } } while (0)
#pragma unroll
  for (int n = 0; n < NL; ++n) {
    if (n == 1) { WG_BARRIER(); tb = now32() + LAT + w * (TAU / (16 * NS)); }
    if (n >= WIN) wait_vm<WIN - 1>();
    if (n >= 1) pace(tb, tstep(n));
    K[n] = LDKV(rk, koff, n);
    SB();
  }
#pragma unroll
  for (int n = 0; n < N0; ++n) ISSUE_V(n);
  asm volatile("" :: "v"(cpf));
  LDS_WAIT(L_QF, 1);
  const v4u qc = *(LAS v4u*)(lds + L_Q + (4 * g + (lane & 3)) * 16);
  const u32 epoch = *(LAS u32*)(lds + L_EP);
  v4u qf[4];
#pragma unroll
  for (int t = 0; t < 4; ++t) qf[t] = ((c >> 2) == t) ? qc : v4u{0u, 0u, 0u, 0u};
  float sc[NG];
#pragma unroll
  for (int gi = 0; gi < NG; ++gi) {
    f4 d = {0.f, 0.f, 0.f, 0.f};
#pragma unroll
    for (int t = 0; t < 4; ++t) d = mfma(K[4 * gi + t], qf[t], d);
    sc[gi] = (dpp<0x00>(d[0]) + dpp<0x55>(d[1])) + (dpp<0xAA>(d[2]) + dpp<0xFF>(d[3]));
    OPAQUE(sc[gi]);
    if (gi & 1) { SB(); ISSUE_V(N0 + (gi >> 1)); }
  }
  float m = fmaxf(fmaxf(sc[0], sc[1]), fmaxf(sc[2], sc[3]));
  m = fmaxf(m, dpp<HMIR>(m)); m = rows_max(fmaxf(m, dpp<MIR>(m)));
  u32 pp[NG]; float l = 0.f;
#pragma unroll
  for (int gi = 0; gi < NG; ++gi) { const float p = __builtin_amdgcn_exp2f((sc[gi] - m) * C2); pp[gi] = pk(p, p); l += hi16(pp[gi]); }
  l += dpp<HMIR>(l); l = rows_sum(l + dpp<MIR>(l));
  if (lane == 0) *(LAS f2*)(lds + L_ML + w * 8) = f2{m, l};
  LDS_ARRIVE(L_SM);
  v4u dm;
#pragma unroll
  for (int q = 0; q < 4; ++q) dm[q] = ((c & 2) == 0 && (c >> 2) == q) ? ((c & 1) ? 0xffff0000u : 0x0000ffffu) : 0u;
  f4 acc = {0.f, 0.f, 0.f, 0.f};
#define PV(n) do { const u32 pb = ((n) & 3) == 0 ? dppu<BCAST + 0>(pp[(n) >> 2]) : ((n) & 3) == 1 ? dppu<BCAST + 4>(pp[(n) >> 2]) \
                                : ((n) & 3) == 2 ? dppu<BCAST + 8>(pp[(n) >> 2]) : dppu<BCAST + 12>(pp[(n) >> 2]); \
    acc = mfma(v4u{pb & dm[0], pb & dm[1], pb & dm[2], pb & dm[3]}, V[n], acc); SB(); } while (0)
  int done = 0;
#pragma unroll
  for (int n = N0 + NG / 2; n < NL; ++n) {
    ISSUE_V(n);
#pragma unroll
    for (int k = 0; k < 3; ++k) if (done <= n - WIN) { PV(done); ++done; }
  }
  WG_BARRIER();
  u32 aoff[2], zoff[2];
  {
    const int nr = h < 12 ? 24 : 20;
#pragma unroll
    for (int hf = 0; hf < 2; ++hf) {
      const int k = 8 * g + 4 * hf + ((lane >> 2) & 3), kz = k < nr ? k : nr - 1;
      aoff[hf] = L_WO + k * 256 + (((2 * w + ((lane >> 1) & 1)) ^ (2 * (k & 3) + (k & 8))) * 16) + (lane & 1) * 8;
      zoff[hf] = L_W2 + kz * 256 + (((2 * w + ((lane >> 1) & 1)) ^ (2 * (kz & 3) + (kz & 8))) * 16) + (lane & 1) * 8;
      OPAQUE(aoff[hf]); OPAQUE(zoff[hf]);
    }
  }
  float scale = __builtin_amdgcn_exp2f((m - oct_max(*(LAS float*)(lds + L_ML + (lane & 7) * 8))) * C2);
  v4u hb = *(LAS v4u*)(lds + L_HP + g * 16);
  OPAQUE(scale); OPAQUE(hb);
  SB();
#pragma unroll
  for (int n = 0; n < NL; ++n) if (n >= done) PV(n);
  *(LAS f2*)(lds + L_OW + w * 512 + lane * 8) = f2{acc[0] * scale, acc[1] * scale};
  LDS_ARRIVE(L_CNT);
  if (w == 0) {
    LDS_WAIT(L_CNT, NS);
    float o0 = 0.f, o1 = 0.f;
#pragma unroll
    for (int k = 0; k < NS; ++k) { const f2 t = *(LAS f2*)(lds + L_OW + k * 512 + lane * 8); o0 += t[0]; o1 += t[1]; }
    const u32 e0 = enc(o0, epoch + TG_B), e1 = enc(o1, epoch + TG_B);
    st16(rws, (c & 1) ? OOB : 128 + ((c >> 3) * 32 + g * 8 + (c & 7)) * 8, WS_H1 + (h * 8 + s) * H1_STR, v4u{e0, e1, dppu<QX1>(e0), dppu<QX1>(e1)});
    SB();
  }
  LDS_WAIT(L_WF, 1);
  v4u A[2]; u32 ez0, ez1;
  {
    const v2u z0 = trd(lds + zoff[0]), z1 = trd(lds + zoff[1]);
#pragma unroll
    for (int st = 0; st < 2; ++st) { const v2u p = trd(lds + aoff[0] + st * 8192), q = trd(lds + aoff[1] + st * 8192); A[st] = v4u{p[0], p[1], q[0], q[1]}; OPAQUE(A[st]); }
    const f4 az = mfma(v4u{z0[0], z0[1], z1[0], z1[1]}, hb, f4{0.f, 0.f, 0.f, 0.f});
    ez0 = enc((c & 1) ? az[2] : az[0], epoch + TG_C); ez1 = enc((c & 1) ? az[3] : az[1], epoch + TG_D);
  }
  u32 yo = c < 2 ? w * 128 + g * 32 + c * 16 : OOB;
  u32 tgy = (epoch + TG_C) & TM;
  asm volatile("" : "+v"(ez0), "+v"(ez1), "+v"(yo), "+v"(tgy));
  SB();
  v4u o0, o1;
  for (u32 sp = 0;; ++sp) {
    const u32 fl = *(volatile LAS u32*)(lds + L_OF);
    o0 = *(volatile LAS v4u*)(lds + L_OT + g * 16); o1 = *(volatile LAS v4u*)(lds + L_OT + 64 + g * 16);
    if (fl != 0u || sp > (SPIN_CAP << 6)) break;
  }
  SB();
  const f4 ay = mfma(A[1], o1, mfma(A[0], o0, f4{0.f, 0.f, 0.f, 0.f}));
  st16(rws, yo, WS_H0 + (h * 8 + s) * 1024, v4u{(f2u((c & 1) ? ay[2] : ay[0]) & ~TM) | tgy, (f2u((c & 1) ? ay[3] : ay[1]) & ~TM) | tgy, ez0, ez1});
}

extern "C" void kernel_launch(void* const* d_in, const int* in_sizes, int n_in, void* d_out, int out_size, void* d_ws, size_t ws_size,
                              hipStream_t stream) {
  fused_decode<<<256, NTHR, 0, stream>>>((const u16*)d_in[0], (const u16*)d_in[3], (const u16*)d_in[6], (unsigned char*)d_ws,
                                         (const u16*)d_in[1], (const u16*)d_in[2], (const u16*)d_in[4], (const u16*)d_in[8],
                                         (const u16*)d_in[7], (const u16*)d_in[9], (u16*)d_out);
}
```
